```python
import torch

def _softmax(x, axis=-1):
    return torch.softmax(x, dim=axis)
def _gelu(x):
    return torch.nn.functional.gelu(x, approximate="tanh")
def _silu(x):
    return torch.nn.functional.silu(x)
def _sigmoid(x):
    return torch.sigmoid(x)
def _layernorm(x, axis=-1, eps=1e-5):
    m = x.mean(dim=axis, keepdim=True)
    v = x.var(dim=axis, keepdim=True, unbiased=False)
    return (x - m) / torch.sqrt(v + eps)
def _scatter_add(tgt, idx, src, axis=0):
    return tgt.clone().index_add_(axis, idx, src)
def _segment_sum(src, seg, n):
    out = torch.zeros((n,) + src.shape[1:], dtype=src.dtype)
    return out.index_add_(0, seg, src)
def _pad(x, pw):
    flat = tuple(v for pair in reversed(pw) for v in pair)
    return torch.nn.functional.pad(x, flat)

kernel_name = 'parallel_sum__gqa_block_cached__glu_gated_swiglu__decode'

def setup_inputs():

    _w = (lambda a: (lambda t: t.to(torch.bfloat16) if t.is_floating_point() else t)(torch.as_tensor(a)))
    def _A_setup():
        B, Sq, D = 1, 1, 640
        Hq, Hkv, Sk, Dh = 32, 32, 32768, 128
        assert Hq % Hkv == 0, (Hq, Hkv)
        return {
            "x": _w((lambda *s: torch.randn(*s, dtype=torch.bfloat16) * 0.05)(B, Sq, D)),
            "k_cache": _w((lambda *s: torch.randn(*s, dtype=torch.bfloat16) * 3.2)(B, Hkv, Sk, Dh)),
            "v_cache": _w((lambda *s: torch.randn(*s, dtype=torch.bfloat16) * 1.6)(B, Hkv, Sk, Dh)),
            "wq": _w((lambda *s: torch.randn(*s, dtype=torch.bfloat16) * 3.2)(D, Hq * Dh) * (D ** -0.5)),
            "wo": _w((lambda *s: torch.randn(*s, dtype=torch.bfloat16) * 1.6)(Hq * Dh, D) * ((Hq * Dh) ** -0.5)),
        }
    _A_in = _A_setup()

    def _B_setup():
        B, S, D, F = 1, 1, 640, 1728
        return {
            "x": _w((lambda *s: torch.randn(*s, dtype=torch.bfloat16) * 0.05)(B, S, D)),
            "w1": _w((lambda *s: torch.randn(*s, dtype=torch.bfloat16) * 1.6)(D, F) * (2.0 / D) ** 0.5),
            "w3": _w((lambda *s: torch.randn(*s, dtype=torch.bfloat16) * 1.6)(D, F) * (2.0 / D) ** 0.5),
            "w2": _w((lambda *s: torch.randn(*s, dtype=torch.bfloat16) * 1.6)(F, D) * (2.0 / F) ** 0.5),
        }
    _B_in = _B_setup()

    return {
        "A_x": _A_in["x"],
        "A_k_cache": _A_in["k_cache"],
        "A_v_cache": _A_in["v_cache"],
        "A_wq": _A_in["wq"],
        "A_wo": _A_in["wo"],
        "B_x": _B_in["x"],
        "B_w1": _B_in["w1"],
        "B_w3": _B_in["w3"],
        "B_w2": _B_in["w2"],
    }

def reference(A_x, A_k_cache, A_v_cache, A_wq, A_wo, B_x, B_w1, B_w3, B_w2):
    def _A_ref(x, k_cache, v_cache, wq, wo):
        B, Sq, D = x.shape
        Hq, Dh = 32, 128
        Hkv = k_cache.shape[1]
        rep = Hq // Hkv
        q = (lambda x, *d: x.permute(*d))((x @ wq).reshape(B, Sq, Hq, Dh), 0, 2, 1, 3)
        k = (lambda x, n, axis: torch.repeat_interleave(x, n, dim=axis))(k_cache, rep, axis=1)
        v = (lambda x, n, axis: torch.repeat_interleave(x, n, dim=axis))(v_cache, rep, axis=1)
        scores = torch.einsum("bhqd,bhkd->bhqk", q, k) * (Dh ** -0.5)
        p = _softmax(scores, axis=-1)
        o = torch.einsum("bhqk,bhkd->bhqd", p, v)
        return (lambda x, *d: x.permute(*d))(o, 0, 2, 1, 3).reshape(B, Sq, Hq * Dh) @ wo

    def _B_ref(x, w1, w3, w2):
        gate = torch.einsum("bsd,df->bsf", x, w1)
        up = torch.einsum("bsd,df->bsf", x, w3)
        h = _silu(gate) * up
        out = torch.einsum("bsf,fd->bsd", h, w2)
        return out

    x = A_x
    xn = _layernorm(x, axis=-1)
    return x + _A_ref(xn, A_k_cache, A_v_cache, A_wq, A_wo) + _B_ref(xn, B_w1, B_w3, B_w2)
```

```cpp
#include <hip/hip_runtime.h>
#include <hip/hip_bf16.h>
#include <stdint.h>

#define MIMIC 1

typedef unsigned short u16;
typedef unsigned u32x4 __attribute__((ext_vector_type(4)));
typedef __bf16 bf16x2 __attribute__((ext_vector_type(2)));

constexpr int D = 640, H = 32, SK = 32768, DH = 128, F = 1728;
constexpr int NSPLIT = 8, KEYS_PER_WG = SK / NSPLIT;

__device__ __forceinline__ float bf2f(u16 v) { return __uint_as_float(((unsigned)v) << 16); }
__device__ __forceinline__ float rbf(float x) {
  return (float)(__bf16)x;
}
__device__ __forceinline__ float dot2(unsigned k, unsigned q, float acc) {
  return __builtin_amdgcn_fdot2_f32_bf16(__builtin_bit_cast(bf16x2, k), __builtin_bit_cast(bf16x2, q), acc, false);
}
template <int CTRL> __device__ __forceinline__ float dpp(float x) {
  return __builtin_bit_cast(float, __builtin_amdgcn_mov_dpp(__builtin_bit_cast(int, x), CTRL, 0xf, 0xf, true));
}
constexpr int XOR1 = 0xB1, XOR2 = 0x4E, XOR7 = 0x141, XOR8 = 0x128;

__device__ void layernorm_to_lds(const u16* x, float* xn, float* red) {
  int tid = threadIdx.x;
  if (tid < 64) {
    float v[10]; float s = 0.f;
    for (int i = 0; i < 10; ++i) { v[i] = bf2f(x[tid + 64 * i]); s += v[i]; }
    for (int m = 32; m >= 1; m >>= 1) s += __shfl_xor(s, m);
    float mean = s * (1.f / D);
    float q = 0.f;
    for (int i = 0; i < 10; ++i) { float d = v[i] - mean; q += d * d; }
    for (int m = 32; m >= 1; m >>= 1) q += __shfl_xor(q, m);
    float var = rbf(q * (1.f / D));
    float mb = rbf(mean);
    float den = rbf(sqrtf(rbf(var + 1e-5f)));
    for (int i = 0; i < 10; ++i) xn[tid + 64 * i] = rbf(rbf(v[i] - mb) / den);
  }
  __syncthreads();
}

__global__ void __launch_bounds__(256) k1(const u16* x, const u16* wq, const u16* w1, const u16* w3, float* qout, float* hout) {
  __shared__ float xn[D]; __shared__ float red[2][4][128];
  layernorm_to_lds(x, xn, nullptr);
  int tid = threadIdx.x, b = blockIdx.x;
  if (b < H) {
    int col = tid & 127, part = tid >> 7; float acc = 0.f;
    for (int r = part; r < D; r += 2) acc += xn[r] * bf2f(wq[(size_t)r * (H * DH) + b * DH + col]);
    red[0][part][col] = acc; __syncthreads();
    if (tid < 128) qout[b * DH + tid] = rbf(red[0][0][tid] + red[0][1][tid]);
  } else {
    int fb = b - H; int col = tid & 63, part = tid >> 6; float g = 0.f, u = 0.f;
    for (int r = part; r < D; r += 4) { g += xn[r] * bf2f(w1[(size_t)r * F + fb * 64 + col]); u += xn[r] * bf2f(w3[(size_t)r * F + fb * 64 + col]); }
    red[0][part][col] = g; red[1][part][col] = u; __syncthreads();
    if (tid < 64) {
      float gg = rbf(red[0][0][tid] + red[0][1][tid] + red[0][2][tid] + red[0][3][tid]);
      float uu = rbf(red[1][0][tid] + red[1][1][tid] + red[1][2][tid] + red[1][3][tid]);
      float s = rbf(gg / (1.f + __expf(-gg)));
      hout[fb * 64 + tid] = rbf(s * uu);
    }
  }
}

constexpr int WAVES = 16;
__global__ void __launch_bounds__(1024) k2(const u16* kc, const u16* vc, const float* q, float* part) {
  __shared__ float sm[WAVES * 4][132];
  int tid = threadIdx.x, lane = tid & 63, wave = tid >> 6, r = lane >> 4, c = lane & 15;
  int h = blockIdx.x / NSPLIT, s = blockIdx.x % NSPLIT;
  unsigned qp[4];
  for (int i = 0; i < 4; ++i) { float a = q[h * DH + 8 * c + 2 * i], b = q[h * DH + 8 * c + 2 * i + 1];
    qp[i] = (__float_as_uint((float)(__bf16)a) >> 16) | (__float_as_uint((float)(__bf16)b) & 0xffff0000u); }
  const size_t base = ((size_t)h * SK + (size_t)s * KEYS_PER_WG) * DH;
  float m = -INFINITY, l = 0.f, acc[8];
  for (int i = 0; i < 8; ++i) acc[i] = 0.f;
  constexpr float SC = 0.08838834764831845f;
  const float scb = rbf(SC);
#pragma unroll 1
  for (int blk = wave; blk < KEYS_PER_WG / 32; blk += WAVES) {
    const u32x4* kp = (const u32x4*)(kc + base + (size_t)blk * 32 * DH) + lane;
    const u32x4* vp = (const u32x4*)(vc + base + (size_t)blk * 32 * DH) + lane;
    u32x4 K[8], V[8];
#pragma unroll
    for (int t = 0; t < 8; ++t) K[t] = __builtin_nontemporal_load(kp + t * 64);
#pragma unroll
    for (int t = 0; t < 8; ++t) V[t] = __builtin_nontemporal_load(vp + t * 64);
    float x[8];
#pragma unroll
    for (int t = 0; t < 8; ++t) { float d = 0.f;
#pragma unroll
      for (int k = 0; k < 4; ++k) d = dot2(K[t][k], qp[k], d);
      d += dpp<XOR8>(d); d += dpp<XOR7>(d); d += dpp<XOR2>(d); d += dpp<XOR1>(d);
      x[t] = rbf(rbf(d) * SC) * 1.4426950408889634f; }
    float pm = x[0];
#pragma unroll
    for (int t = 1; t < 8; ++t) pm = fmaxf(pm, x[t]);
    float mn = fmaxf(m, pm); float al = __builtin_amdgcn_exp2f(m - mn); m = mn;
    l *= al;
#pragma unroll
    for (int k = 0; k < 8; ++k) acc[k] *= al;
#pragma unroll
    for (int t = 0; t < 8; ++t) { float p = __builtin_amdgcn_exp2f(x[t] - mn); l += p;
#pragma unroll
      for (int k = 0; k < 4; ++k) { acc[2 * k] = fmaf(p, __uint_as_float(V[t][k] << 16), acc[2 * k]); acc[2 * k + 1] = fmaf(p, __uint_as_float(V[t][k] & 0xffff0000u), acc[2 * k + 1]); } }
  }
  (void)scb;
  float* rec = sm[wave * 4 + r];
  for (int k = 0; k < 8; ++k) rec[4 + 8 * c + k] = acc[k];
  if (c == 0) { rec[0] = m; rec[1] = l; }
  __syncthreads();
  if (tid < 128) {
    float M = -INFINITY;
#pragma unroll 4
    for (int w = 0; w < WAVES * 4; ++w) M = fmaxf(M, sm[w][0]);
    float L = 0.f, o = 0.f;
#pragma unroll 4
    for (int w = 0; w < WAVES * 4; ++w) { float wt = __builtin_amdgcn_exp2f(sm[w][0] - M); L = fmaf(wt, sm[w][1], L); o = fmaf(wt, sm[w][4 + tid], o); }
    float* out = part + (size_t)blockIdx.x * 132;
    out[4 + tid] = o; if (tid == 0) { out[0] = M; out[1] = L; }
  }
}

__global__ void __launch_bounds__(1024) k3(const u16* x, const float* part, const float* hv, const u16* wo, const u16* w2, u16* out) {
  __shared__ float o[H * DH]; __shared__ float red[2][64][16];
  int tid = threadIdx.x;
  for (int i = tid; i < H * DH; i += 1024) { int h = i >> 7, d = i & 127; float M = -INFINITY;
    for (int s = 0; s < NSPLIT; ++s) M = fmaxf(M, part[(h * NSPLIT + s) * 132]);
    float L = 0.f, a = 0.f;
    for (int s = 0; s < NSPLIT; ++s) { const float* p = part + (h * NSPLIT + s) * 132; float wt = __builtin_amdgcn_exp2f(p[0] - M); L = fmaf(wt, p[1], L); a = fmaf(wt, p[4 + d], a); }
    o[i] = rbf(a / L); }
  __syncthreads();
  int col = blockIdx.x * 16 + (tid & 15), rg = tid >> 4; float a = 0.f, f = 0.f;
  for (int r = rg; r < H * DH; r += 64) a = fmaf(o[r], bf2f(wo[(size_t)r * D + col]), a);
  for (int r = rg; r < F; r += 64) f = fmaf(hv[r], bf2f(w2[(size_t)r * D + col]), f);
  red[0][rg][tid & 15] = a; red[1][rg][tid & 15] = f; __syncthreads();
  if (tid < 16) { float A = 0.f, B = 0.f; for (int i = 0; i < 64; ++i) { A += red[0][i][tid]; B += red[1][i][tid]; }
    float res = rbf(bf2f(x[col]) + rbf(A)) + rbf(B);
    __bf16 rb = (__bf16)res; out[col] = __builtin_bit_cast(u16, rb); }
}

extern "C" void kernel_launch(void* const* d_in, const int* in_sizes, int n_in, void* d_out, int out_size, void* d_ws, size_t ws_size, hipStream_t stream) {
  const u16* x = (const u16*)d_in[0]; const u16* kc = (const u16*)d_in[1]; const u16* vc = (const u16*)d_in[2];
  const u16* wq = (const u16*)d_in[3]; const u16* wo = (const u16*)d_in[4];
  const u16* w1 = (const u16*)d_in[6]; const u16* w3 = (const u16*)d_in[7]; const u16* w2 = (const u16*)d_in[8];
  float* q = (float*)d_ws; float* hv = q + 4096; float* part = hv + 2048;
  k1<<<H + F / 64, 256, 0, stream>>>(x, wq, w1, w3, q, hv);
  k2<<<H * NSPLIT, 1024, 0, stream>>>(kc, vc, q, part);
  k3<<<D / 16, 1024, 0, stream>>>(x, part, hv, wo, w2, (u16*)d_out);
}
```

```cpp
#include <hip/hip_runtime.h>
#include <hip/hip_bf16.h>
#include <stdint.h>

#define WIN 20
#define WQ_AUX 2
#define USE_MEMSET 0

#define GAS __attribute__((address_space(1)))
#define LAS __attribute__((address_space(3)))
typedef unsigned short u16;
typedef unsigned long long u64;
typedef unsigned u32x2 __attribute__((ext_vector_type(2)));
typedef unsigned u32x4 __attribute__((ext_vector_type(4)));
typedef float f32x2 __attribute__((ext_vector_type(2)));
typedef float f32x4 __attribute__((ext_vector_type(4)));
typedef __bf16 bf16x2 __attribute__((ext_vector_type(2)));
typedef GAS u64 gu64;
typedef GAS unsigned gu32;

constexpr int D = 640, H = 32, SK = 32768, DH = 128, F = 1728;
constexpr int NS = 8;
constexpr int FH = F / H;
constexpr int CS = D / NS;
constexpr unsigned HEADB = SK * DH * 2;
constexpr int NTHR = 384;

constexpr int L_RING = 0;
constexpr int L_SC = 131072;
constexpr int L_XN = L_SC + 16384;
constexpr int L_QP = L_XN + 1280;
constexpr int L_R2 = L_QP + 2048;
constexpr int L_FFN = L_R2 + 1280;
constexpr int L_HV = L_FFN + 320;
constexpr int L_ML = L_HV + 128;
constexpr int L_CW = L_ML + 32;
constexpr int L_DONE = L_CW + 16;
constexpr int L_FULL = L_DONE + 16;
constexpr int L_END = L_FULL + 16;

constexpr size_t W_EPOCH = 0;
constexpr size_t W_X0 = 4096;
constexpr size_t W_X1 = W_X0 + 256 * 1024;
constexpr size_t W_X3 = W_X1 + 4096;
constexpr size_t W_X4 = W_X3 + 256 * 1024;
constexpr size_t W_END = W_X4 + 256 * 640;
constexpr size_t W_TS = 16u << 20;

constexpr unsigned SPIN_LDS = 1u << 22, SPIN_MEM = 1u << 17;

#define RLX_AGENT __ATOMIC_RELAXED, __HIP_MEMORY_SCOPE_AGENT

__device__ __forceinline__ float rbf(float x) { return (float)(__bf16)x; }
__device__ __forceinline__ float lo16(unsigned v) { return __uint_as_float(v << 16); }
__device__ __forceinline__ float hi16(unsigned v) { return __uint_as_float(v & 0xffff0000u); }
__device__ __forceinline__ unsigned pack2(float a, float b) { bf16x2 v = {(__bf16)a, (__bf16)b}; return __builtin_bit_cast(unsigned, v); }
__device__ __forceinline__ float dot2(unsigned a, unsigned b, float c) {
  return __builtin_amdgcn_fdot2_f32_bf16(__builtin_bit_cast(bf16x2, a), __builtin_bit_cast(bf16x2, b), c, false);
}
__device__ __forceinline__ unsigned pair_lo(unsigned r0, unsigned r1) { return __builtin_amdgcn_perm(r1, r0, 0x05040100u); }
__device__ __forceinline__ unsigned pair_hi(unsigned r0, unsigned r1) { return __builtin_amdgcn_perm(r1, r0, 0x07060302u); }
template <int CTRL> __device__ __forceinline__ float dpp(float x) {
  return __builtin_bit_cast(float, __builtin_amdgcn_mov_dpp(__builtin_bit_cast(int, x), CTRL, 0xf, 0xf, true));
}
constexpr int XOR1 = 0xB1, XOR2 = 0x4E, HMIR = 0x141, ROR4 = 0x124, ROR8 = 0x128, BCAST = 0x150;
__device__ __forceinline__ float x16_add(float x) { auto s = __builtin_amdgcn_permlane16_swap(__float_as_uint(x), __float_as_uint(x), false, false); return __uint_as_float(s[0]) + __uint_as_float(s[1]); }
__device__ __forceinline__ float x32_add(float x) { auto s = __builtin_amdgcn_permlane32_swap(__float_as_uint(x), __float_as_uint(x), false, false); return __uint_as_float(s[0]) + __uint_as_float(s[1]); }
__device__ __forceinline__ float x16_max(float x) { auto s = __builtin_amdgcn_permlane16_swap(__float_as_uint(x), __float_as_uint(x), false, false); return fmaxf(__uint_as_float(s[0]), __uint_as_float(s[1])); }
__device__ __forceinline__ float x32_max(float x) { auto s = __builtin_amdgcn_permlane32_swap(__float_as_uint(x), __float_as_uint(x), false, false); return fmaxf(__uint_as_float(s[0]), __uint_as_float(s[1])); }
__device__ __forceinline__ float row_sum(float x) { x += dpp<XOR1>(x); x += dpp<XOR2>(x); x += dpp<HMIR>(x); x += dpp<ROR8>(x); return x; }
__device__ __forceinline__ float row_max(float x) { x = fmaxf(x, dpp<XOR1>(x)); x = fmaxf(x, dpp<XOR2>(x)); x = fmaxf(x, dpp<HMIR>(x)); x = fmaxf(x, dpp<ROR8>(x)); return x; }
__device__ __forceinline__ float wave_sum(float x) { return x32_add(x16_add(row_sum(x))); }
__device__ __forceinline__ float wave_max(float x) { return x32_max(x16_max(row_max(x))); }
__device__ __forceinline__ void put(gu64* g, unsigned tag, float v) { __hip_atomic_store(g, ((u64)tag << 32) | __float_as_uint(v), RLX_AGENT); }
template <int N> __device__ __forceinline__ void gather(gu64* g, int S, unsigned tag, float (&v)[N]) {
  for (unsigned spins = 0; spins < SPIN_MEM; ++spins) {
    bool ok = true;
#pragma unroll
    for (int k = 0; k < N; ++k) { const u64 x = __hip_atomic_load(g + k * S, RLX_AGENT); v[k] = __uint_as_float((unsigned)x); ok &= (unsigned)(x >> 32) == tag; }
    if (__all(ok)) break;
  }
}
__device__ __forceinline__ unsigned lds_peek(unsigned addr) {
  unsigned v; asm volatile("ds_read_b32 %0, %1\n\ts_waitcnt lgkmcnt(0)" : "=v"(v) : "v"(addr) : "memory");
  return __builtin_amdgcn_readfirstlane(v);
}
template <int SLEEP> __device__ __forceinline__ unsigned lds_wait_ge(unsigned addr, unsigned want) {
  unsigned v = lds_peek(addr);
  for (unsigned spins = 0; v < want && spins < SPIN_LDS; ++spins) { __builtin_amdgcn_s_sleep(SLEEP); v = lds_peek(addr); }
  return v < want ? want : v;
}
__device__ __forceinline__ void lds_post(unsigned addr, unsigned v) { asm volatile("ds_write_b32 %0, %1" :: "v"(addr), "v"(v) : "memory"); }
__device__ __forceinline__ void lds_arrive(unsigned addr, int lane) {
  if (lane == 0) asm volatile("ds_add_u32 %0, %1" :: "v"(addr), "v"(1u) : "memory");
}
__device__ __forceinline__ void wg_barrier() { asm volatile("s_waitcnt lgkmcnt(0)\n\ts_barrier" ::: "memory"); }
__device__ __forceinline__ __amdgpu_buffer_rsrc_t rsrc(const void* p, unsigned bytes) { return __builtin_amdgcn_make_buffer_rsrc((void*)p, 0, bytes, 0x00020000); }

__device__ __forceinline__ void layernorm_to_lds(const unsigned (&xr)[5], LAS unsigned* xn, int lane) {
  float s = 0.f;
#pragma unroll
  for (int i = 0; i < 5; ++i) s += lo16(xr[i]) + hi16(xr[i]);
  const float mean = wave_sum(s) * (1.f / D);
  float q = 0.f;
#pragma unroll
  for (int i = 0; i < 5; ++i) { const float a = lo16(xr[i]) - mean, b = hi16(xr[i]) - mean; q += a * a + b * b; }
  const float var = rbf(wave_sum(q) * (1.f / D)), mb = rbf(mean);
  const float den = rbf(__builtin_sqrtf(rbf(var + 1e-5f)));
#pragma unroll
  for (int i = 0; i < 5; ++i) xn[lane + 64 * i] = pack2(rbf(lo16(xr[i]) - mb) / den, rbf(hi16(xr[i]) - mb) / den);
}

#define STAMP(i) do {} while (0)

__global__ void __launch_bounds__(NTHR, 2)
fused_decode(const u16* __restrict__ x, const u16* __restrict__ kc, const u16* __restrict__ vc, const u16* __restrict__ wq,
             const u16* __restrict__ wo, const u16* __restrict__ w1, const u16* __restrict__ w3, const u16* __restrict__ w2,
             u16* __restrict__ out, unsigned char* ws_) {
  __shared__ __attribute__((aligned(16))) unsigned char smem[L_END];
  LAS unsigned char* lds = (LAS unsigned char*)smem;
  const unsigned lbase = (unsigned)(size_t)lds;
  GAS unsigned char* ws = (GAS unsigned char*)ws_;
  const int lane = threadIdx.x & 63, wave = __builtin_amdgcn_readfirstlane(threadIdx.x >> 6);
  const int b = blockIdx.x, h = (b & 7) * 4 + (b >> 6), s = (b >> 3) & 7, wg = h * NS + s;

  if (wave == 5 && lane < 8) ((LAS unsigned*)(lds + L_DONE))[lane] = 0u;
  wg_barrier();

  if (wave == 0) {
    __builtin_amdgcn_s_setprio(3);
    const __amdgpu_buffer_rsrc_t rk = rsrc((const char*)kc + (size_t)h * HEADB, HEADB), rv = rsrc((const char*)vc + (size_t)h * HEADB, HEADB);
    const unsigned voff = lane * 16, a_full = lbase + L_FULL, a_done = lbase + L_DONE;
    STAMP(0);
#pragma unroll 1
    for (int g = 0; g < 128; ++g) {
      const __amdgpu_buffer_rsrc_t rs = g < 64 ? rk : rv;
      const unsigned so = ((g + 2 * h) & 63) * (NS * 16384u) + s * 16384u;
      LAS unsigned char* dst = lds + L_RING + (g & 7) * 16384;
      u32x4 dn;
#pragma unroll
      for (int q = 0; q < 4; ++q) {
#define DMA(k) do { \
          __builtin_amdgcn_raw_ptr_buffer_load_lds(rs, dst + q * 4096, 16, voff, so + q * 4096, (k) * 1024, 2  ); \
          asm volatile("s_waitcnt vmcnt(%0)" :: "i"(WIN) : "memory"); \
          if (((k) + 1 + 64 - WIN) % 4 == 0) { const int n = 4 * g + (4 * q + (k) + 1 + 64 - WIN) / 4 - 16; lds_post(a_full, n > 0 ? n : 0); } } while (0)
        DMA(0); DMA(1); DMA(2); DMA(3);
#undef DMA
        if (q == 1) asm volatile("ds_read_b128 %0, %1" : "=v"(dn) : "v"(a_done) : "memory");
      }
      asm volatile("s_waitcnt lgkmcnt(0)" : "+v"(dn) :: "memory");
      if (g >= 7 && g < 127) {
        unsigned m = __builtin_amdgcn_readfirstlane(min(min(dn.x, dn.y), min(dn.z, dn.w)));
        for (unsigned spins = 0; m < (unsigned)(g - 6) && spins < SPIN_LDS; ++spins) {
          asm volatile("ds_read_b128 %0, %1\n\ts_waitcnt lgkmcnt(0)" : "=v"(dn) : "v"(a_done) : "memory");
          m = __builtin_amdgcn_readfirstlane(min(min(dn.x, dn.y), min(dn.z, dn.w)));
        }
      }
    }
#pragma unroll
    for (int n = (WIN - 1) / 4; n >= 0; --n) { asm volatile("s_waitcnt vmcnt(%0)" :: "i"(4 * n) : "memory"); lds_post(a_full, 512 - n); }
    STAMP(3);
    return;
  }

  asm volatile("" :: "s"(x), "s"(wq), "s"(wo), "s"(w1), "s"(w3), "s"(w2), "s"(out), "s"(ws));
  unsigned xr[5];
#pragma unroll
  for (int i = 0; i < 5; ++i) xr[i] = ((const GAS unsigned*)x)[lane + 64 * i];
  unsigned ep = USE_MEMSET ? 0u : __hip_atomic_load((gu32*)(ws + W_EPOCH), RLX_AGENT);
  LAS unsigned* xn = (LAS unsigned*)(lds + L_XN);
#define PIN_X() asm volatile("" : "+v"(xr[0]), "+v"(xr[1]), "+v"(xr[2]), "+v"(xr[3]), "+v"(xr[4]), "+v"(ep))

  if (wave == 5) {
    PIN_X();
    const unsigned tag = __builtin_amdgcn_readfirstlane(ep) + 1u;
    layernorm_to_lds(xr, xn, lane);
    const int half = lane >> 5, j = lane & 31, jc = j < FH / 2 ? j : FH / 2 - 1;
    unsigned xp[20];
#pragma unroll
    for (int i = 0; i < 5; ++i) { const u32x4 t = *(LAS u32x4*)(xn + (CS * s + 40 * half) / 2 + 4 * i); xp[4 * i] = t.x; xp[4 * i + 1] = t.y; xp[4 * i + 2] = t.z; xp[4 * i + 3] = t.w; }
    lds_wait_ge<8>(lbase + L_FULL + 4, 4);
    float g0 = 0.f, g1 = 0.f, u0 = 0.f, u1 = 0.f;
    {
      const __amdgpu_buffer_rsrc_t r1 = rsrc(w1, D * F * 2), r3 = rsrc(w3, D * F * 2);
      const unsigned vo = (CS * s + 40 * half) * (F * 2) + FH * 2 * h + 4 * jc;
      unsigned a[40], c[40];
#pragma unroll
      for (int i = 0; i < 40; ++i) { a[i] = __builtin_amdgcn_raw_buffer_load_b32(r1, vo, i * (F * 2), 2); c[i] = __builtin_amdgcn_raw_buffer_load_b32(r3, vo, i * (F * 2), 2); }
#pragma unroll
      for (int i = 0; i < 20; ++i) {
        g0 = dot2(pair_lo(a[2 * i], a[2 * i + 1]), xp[i], g0); g1 = dot2(pair_hi(a[2 * i], a[2 * i + 1]), xp[i], g1);
        u0 = dot2(pair_lo(c[2 * i], c[2 * i + 1]), xp[i], u0); u1 = dot2(pair_hi(c[2 * i], c[2 * i + 1]), xp[i], u1);
      }
    }
    g0 = x32_add(g0); g1 = x32_add(g1); u0 = x32_add(u0); u1 = x32_add(u1);
    gu64* x0 = (gu64*)(ws + W_X0);
    put(x0 + wg * 128 + lane, tag, half ? u0 : g0); put(x0 + wg * 128 + 64 + lane, tag, half ? u1 : g1);
    unsigned wd[FH];
    {
      const __amdgpu_buffer_rsrc_t r2 = rsrc(w2, F * D * 2);
      const unsigned vo = FH * h * (D * 2) + CS * 2 * s + 4 * (lane < CS / 2 ? lane : CS / 2 - 1);
#pragma unroll
      for (int i = 0; i < FH; ++i) wd[i] = __builtin_amdgcn_raw_buffer_load_b32(r2, vo, i * (D * 2), 2);
    }
    float v[16];
    gather<16>(x0 + h * NS * 128 + lane, 64, tag, v);
    float t0 = 0.f, t1 = 0.f;
#pragma unroll
    for (int p = 0; p < NS; ++p) { t0 += v[2 * p]; t1 += v[2 * p + 1]; }
    t0 = rbf(t0); t1 = rbf(t1);
    const auto s0 = __builtin_amdgcn_permlane32_swap(__float_as_uint(t0), __float_as_uint(t0), false, false);
    const auto s1 = __builtin_amdgcn_permlane32_swap(__float_as_uint(t1), __float_as_uint(t1), false, false);
    const float ga = __uint_as_float(s0[0]), ua = __uint_as_float(s0[1]), gb = __uint_as_float(s1[0]), ub = __uint_as_float(s1[1]);
    const float ha = rbf(ga / (1.f + __expf(-ga))) * ua, hb = rbf(gb / (1.f + __expf(-gb))) * ub;
    LAS unsigned* hv = (LAS unsigned*)(lds + L_HV);
    hv[j] = pack2(ha, hb);
    float f0 = 0.f, f1 = 0.f;
#pragma unroll
    for (int i = 0; i < FH / 2; ++i) { const unsigned hp = hv[i]; f0 = dot2(pair_lo(wd[2 * i], wd[2 * i + 1]), hp, f0); f1 = dot2(pair_hi(wd[2 * i], wd[2 * i + 1]), hp, f1); }
    if (lane < CS / 2) *(LAS f32x2*)(lds + L_FFN + 8 * lane) = f32x2{f0, f1};
    lds_wait_ge<32>(lbase + L_FULL + 8, 4);
    const f32x2 ml = *(LAS f32x2*)(lds + L_ML + 8 * (lane & 3));
    float mg = fmaxf(ml.x, dpp<XOR1>(ml.x)); mg = fmaxf(mg, dpp<XOR2>(mg));
    float lg = ml.y * __builtin_amdgcn_exp2f(ml.x - mg); lg += dpp<XOR1>(lg); lg += dpp<XOR2>(lg);
    gu64* x1 = (gu64*)(ws + W_X1) + h * 16;
    if (lane < 2) put(x1 + 2 * s + lane, tag, lane ? lg : mg);
    float e[2];
    gather<2>(x1 + 2 * (lane & 7), 1, tag, e);
    float M = fmaxf(e[0], dpp<XOR1>(e[0])); M = fmaxf(M, dpp<XOR2>(M)); M = fmaxf(M, dpp<HMIR>(M));
    float L = e[1] * __builtin_amdgcn_exp2f(e[0] - M); L += dpp<XOR1>(L); L += dpp<XOR2>(L); L += dpp<HMIR>(L);
    if (lane < 4) *(LAS float*)(lds + L_CW + 4 * lane) = __builtin_amdgcn_exp2f(ml.x - M) / L;
    STAMP(9);
    return;
  }

  const int cw = wave - 1, r = lane >> 4, c = lane & 15;
  unsigned qp[4];
  {
    const __amdgpu_buffer_rsrc_t rq = rsrc(wq, D * H * DH * 2);
    const unsigned vo = (160 * cw + 40 * r) * (H * DH * 2) + h * (DH * 2) + c * 16;
    u32x4 w[40];
#pragma unroll
    for (int i = 0; i < 40; ++i) w[i] = __builtin_amdgcn_raw_buffer_load_b128(rq, vo, i * (H * DH * 2), WQ_AUX);
    __builtin_amdgcn_sched_barrier(0);
    PIN_X();
    layernorm_to_lds(xr, xn, lane);
    unsigned xp[20];
#pragma unroll
    for (int i = 0; i < 5; ++i) { const u32x4 t = *(LAS u32x4*)(xn + (160 * cw + 40 * r) / 2 + 4 * i); xp[4 * i] = t.x; xp[4 * i + 1] = t.y; xp[4 * i + 2] = t.z; xp[4 * i + 3] = t.w; }
    float a[8];
#pragma unroll
    for (int k = 0; k < 8; ++k) a[k] = 0.f;
#pragma unroll
    for (int i = 0; i < 20; ++i)
#pragma unroll
      for (int k = 0; k < 4; ++k) { a[2 * k] = dot2(pair_lo(w[2 * i][k], w[2 * i + 1][k]), xp[i], a[2 * k]); a[2 * k + 1] = dot2(pair_hi(w[2 * i][k], w[2 * i + 1][k]), xp[i], a[2 * k + 1]); }
#pragma unroll
    for (int k = 0; k < 8; ++k) a[k] = x32_add(x16_add(a[k]));
    LAS f32x4* qpart = (LAS f32x4*)(lds + L_QP);
    if (r < 2) qpart[cw * 32 + 2 * c + r] = r ? f32x4{a[4], a[5], a[6], a[7]} : f32x4{a[0], a[1], a[2], a[3]};
    lds_arrive(lbase + L_FULL + 4, lane);
    lds_wait_ge<1>(lbase + L_FULL + 4, 4);
    f32x4 q0 = qpart[2 * c], q1 = qpart[2 * c + 1];
#pragma unroll
    for (int w4 = 1; w4 < 4; ++w4) { q0 += qpart[w4 * 32 + 2 * c]; q1 += qpart[w4 * 32 + 2 * c + 1]; }
    qp[0] = pack2(q0.x, q0.y); qp[1] = pack2(q0.z, q0.w); qp[2] = pack2(q1.x, q1.y); qp[3] = pack2(q1.z, q1.w);
  }
  if (cw == 0) STAMP(1);
  const unsigned tag = __builtin_amdgcn_readfirstlane(ep) + 1u;
  const int col = CS * s + 4 * (h < CS / 4 ? h : 0) + 2 * (lane & 1);
  unsigned xv = ((const GAS unsigned*)x)[col >> 1];
  const int p = lane >> 3, j = lane & 7;
  unsigned wr[4][5];
  {
    const __amdgpu_buffer_rsrc_t ro = rsrc(wo, H * DH * D * 2);
    const unsigned vo = (DH * h + 32 * cw + 4 * j) * (D * 2) + CS * 2 * s + 20 * p;
#pragma unroll
    for (int k = 0; k < 4; ++k)
#pragma unroll
      for (int t = 0; t < 5; ++t) wr[k][t] = __builtin_amdgcn_raw_buffer_load_b32(ro, vo, k * (D * 2) + 4 * t, 2);
  }
  const unsigned a_full = lbase + L_FULL, a_done = lbase + L_DONE + 4 * cw;
  LAS unsigned char* ring = lds + L_RING + cw * 4096 + lane * 16;
  LAS float* sc = (LAS float*)(lds + L_SC) + cw * 1024 + lane;
  unsigned have = 0;
  float mx = -INFINITY;
#pragma unroll 1
  for (int G = 0; G < 16; ++G) {
    float sreg = 0.f;
#pragma unroll
    for (int u = 0; u < 4; ++u) {
      const int g = 4 * G + u;
      if (have < (unsigned)(4 * g + cw + 1)) have = lds_wait_ge<1>(a_full, 4 * g + cw + 1);
      u32x4 kk[4];
#pragma unroll
      for (int i = 0; i < 4; ++i) kk[i] = *(LAS u32x4*)(ring + (g & 7) * 16384 + i * 1024);
      lds_post(a_done, g + 1);
      float a[4];
#pragma unroll
      for (int i = 0; i < 4; ++i) a[i] = dot2(kk[i].w, qp[3], dot2(kk[i].z, qp[2], dot2(kk[i].y, qp[1], dot2(kk[i].x, qp[0], 0.f))));
      const bool b0 = c & 1, b1 = c & 2;
      const float r01 = (b0 ? a[1] : a[0]) + dpp<XOR1>(b0 ? a[0] : a[1]);
      const float r23 = (b0 ? a[3] : a[2]) + dpp<XOR1>(b0 ? a[2] : a[3]);
      float t = (b1 ? r23 : r01) + dpp<XOR2>(b1 ? r01 : r23);
      t += dpp<ROR8>(t); t += dpp<ROR4>(t);
      sreg = (c >> 2) == u ? t : sreg;
    }
    const float s2 = rbf(rbf(sreg) * 0.08838834764831845f) * 1.4426950408889634f;
    mx = fmaxf(mx, s2);
    sc[G * 64] = s2;
  }
  mx = wave_max(mx);
  {
    float l = 0.f;
#pragma unroll
    for (int G = 0; G < 16; ++G) l += __builtin_amdgcn_exp2f(sc[G * 64] - mx);
    l = wave_sum(l);
    if (lane == 0) *(LAS f32x2*)(lds + L_ML + 8 * cw) = f32x2{mx, l};
    lds_arrive(lbase + L_FULL + 8, lane);
  }
  unsigned wp[2][10];
#pragma unroll
  for (int k = 0; k < 2; ++k)
#pragma unroll
    for (int t = 0; t < 5; ++t) {
      wp[k][2 * t] = pair_lo(wr[2 * k][t], wr[2 * k + 1][t]); wp[k][2 * t + 1] = pair_hi(wr[2 * k][t], wr[2 * k + 1][t]);
      asm volatile("" : "+v"(wp[k][2 * t]), "+v"(wp[k][2 * t + 1]));
    }
  asm volatile("" : "+v"(xv));
  float acc[8];
#pragma unroll
  for (int k = 0; k < 8; ++k) acc[k] = 0.f;
#pragma unroll 1
  for (int G = 0; G < 16; ++G) {
    const float preg = __builtin_amdgcn_exp2f(sc[G * 64] - mx);
#define PV_SLOT(u) { \
      const int g = 64 + 4 * G + (u); \
      if (have < (unsigned)(4 * g + cw + 1)) have = lds_wait_ge<1>(a_full, 4 * g + cw + 1); \
      u32x4 vv[4]; \
      _Pragma("unroll") for (int i = 0; i < 4; ++i) vv[i] = *(LAS u32x4*)(ring + (g & 7) * 16384 + i * 1024); \
      lds_post(a_done, g + 1); \
      const float pb[4] = {dpp<BCAST + 4 * (u)>(preg), dpp<BCAST + 4 * (u) + 1>(preg), dpp<BCAST + 4 * (u) + 2>(preg), dpp<BCAST + 4 * (u) + 3>(preg)}; \
      _Pragma("unroll") for (int i = 0; i < 4; ++i) \
        _Pragma("unroll") for (int k = 0; k < 4; ++k) { acc[2 * k] = fmaf(pb[i], lo16(vv[i][k]), acc[2 * k]); acc[2 * k + 1] = fmaf(pb[i], hi16(vv[i][k]), acc[2 * k + 1]); } \
      asm volatile("" : "+v"(acc[0]), "+v"(acc[1]), "+v"(acc[2]), "+v"(acc[3]), "+v"(acc[4]), "+v"(acc[5]), "+v"(acc[6]), "+v"(acc[7])); }
    PV_SLOT(0) PV_SLOT(1) PV_SLOT(2) PV_SLOT(3)
#undef PV_SLOT
  }
  if (cw == 0) STAMP(4);
#pragma unroll
  for (int k = 0; k < 8; ++k) acc[k] = x32_add(x16_add(acc[k]));
  {
    LAS f32x4* part = (LAS f32x4*)(lds + L_QP);
    if (r < 2) part[cw * 32 + 2 * c + r] = r ? f32x4{acc[4], acc[5], acc[6], acc[7]} : f32x4{acc[0], acc[1], acc[2], acc[3]};
  }
  wg_barrier();
  gu64* x3 = (gu64*)(ws + W_X3);
  if (cw < 2) {
    const int t = cw * 64 + lane, d = (t & ~31) + 4 * (t & 7) + ((t >> 3) & 3);
    const f32x4 cs = *(LAS f32x4*)(lds + L_CW);
    LAS float* part = (LAS float*)(lds + L_QP);
    put(x3 + wg * 128 + t, tag, fmaf(cs.w, part[384 + d], fmaf(cs.z, part[256 + d], fmaf(cs.y, part[128 + d], cs.x * part[d]))));
  }
  if (cw == 0) STAMP(5);
  float o[4];
  gather<4>(x3 + (h * NS + p) * 128 + 32 * cw + j, 8, tag, o);
  if (cw == 0) STAMP(6);
#pragma unroll
  for (int k = 0; k < 4; ++k) { o[k] += dpp<ROR8>(o[k]); o[k] = x32_add(x16_add(o[k])); }
  const unsigned o01 = pack2(o[0], o[1]), o23 = pack2(o[2], o[3]);
  float y[10];
#pragma unroll
  for (int t = 0; t < 10; ++t) { y[t] = dot2(wp[1][t], o23, dot2(wp[0][t], o01, 0.f)); y[t] += dpp<XOR1>(y[t]); y[t] += dpp<XOR2>(y[t]); y[t] += dpp<HMIR>(y[t]); }
  if (j == 0) {
#pragma unroll
    for (int t = 0; t < 5; ++t) *(LAS f32x2*)(lds + L_R2 + (cw * CS + 10 * p + 2 * t) * 4) = f32x2{y[2 * t], y[2 * t + 1]};
  }
  wg_barrier();
  gu64* x4 = (gu64*)(ws + W_X4) + s * (H * CS);
  {
    const int t = cw * 64 + lane;
    LAS float* r2 = (LAS float*)(lds + L_R2);
    if (t < CS) put(x4 + h * CS + t, tag, ((r2[t] + r2[CS + t]) + (r2[2 * CS + t] + r2[3 * CS + t])) + ((LAS float*)(lds + L_FFN))[t]);
  }
  if (cw == 0) STAMP(7);
  if (cw == 2 && h < CS / 4) {
    float z[2];
    gather<2>(x4 + (lane >> 1) * CS + 4 * h + 2 * (lane & 1), 1, tag, z);
#pragma unroll
    for (int k = 0; k < 2; ++k) { z[k] += dpp<XOR2>(z[k]); z[k] += dpp<ROR4>(z[k]); z[k] += dpp<ROR8>(z[k]); z[k] = x32_add(x16_add(z[k])); }
    if (lane < 2) ((GAS unsigned*)out)[col >> 1] = pack2(lo16(xv) + z[0], hi16(xv) + z[1]);
    if (!USE_MEMSET && wg == 0 && lane == 0) __hip_atomic_store((gu32*)(ws + W_EPOCH), tag, RLX_AGENT);
    STAMP(8);
  }
}

extern "C" void kernel_launch(void* const* d_in, const int* in_sizes, int n_in, void* d_out, int out_size, void* d_ws, size_t ws_size,
                              hipStream_t stream) {
  if (USE_MEMSET) (void)hipMemsetAsync(d_ws, 0, W_END, stream);
  fused_decode<<<H * NS, NTHR, 0, stream>>>((const u16*)d_in[0], (const u16*)d_in[1], (const u16*)d_in[2], (const u16*)d_in[3], (const u16*)d_in[4],
                                            (const u16*)d_in[6], (const u16*)d_in[7], (const u16*)d_in[8], (u16*)d_out, (unsigned char*)d_ws);
}
```

```cpp
#include <hip/hip_runtime.h>
#include <hip/hip_bf16.h>
#include <stdint.h>

#define GAS __attribute__((address_space(1)))
#define LAS __attribute__((address_space(3)))
typedef unsigned short u16;
typedef unsigned long long u64;
typedef unsigned u32x4 __attribute__((ext_vector_type(4)));
typedef float f32x2 __attribute__((ext_vector_type(2)));
typedef float f32x4 __attribute__((ext_vector_type(4)));
typedef __bf16 bf16x2 __attribute__((ext_vector_type(2)));
typedef GAS u64 gu64;
typedef GAS unsigned gu32;

constexpr int D = 640, H = 32, SK = 32768, DH = 128, F = 1728;
constexpr int NS = 8;
constexpr int FG = F / 64;
constexpr int CG = D / 128;
constexpr unsigned HEADB = SK * DH * 2;
constexpr int NTHR = 384;

constexpr int WIN = 21, W_MIN = 16, W_MAX = 25, PACE_T = 6900, PACE_O = 5, PACE_M0 = 32, LAG0 = -80, LAG_SH = 40;
constexpr int JUMP = 170, KEEP = 40, CENTER = 200;
constexpr int NPRE = 8, XQ_AT = 5;
constexpr int GU_AT = 14;

constexpr int L_RING = 0;
constexpr int L_SC = 131072;
constexpr int L_QP = L_SC + 16384;
constexpr int L_R2 = L_QP + 2048;
constexpr int L_FFN = L_R2 + 2048;
constexpr int L_HV = L_FFN + 512;
constexpr int L_ML = L_HV + 128;
constexpr int L_CW = L_ML + 32;
constexpr int L_DONE = L_CW + 16;
constexpr int L_FULL = L_DONE + 16;
constexpr int L_END = L_FULL + 16;
static_assert(L_FULL == L_DONE + 16 && HEADB == 1u << 23 && W_MIN == 16 && W_MAX == 25, "written into the loader's code");

constexpr size_t W_EPOCH = 0;
constexpr size_t W_REC = 4096;
constexpr size_t W_X1 = W_REC + 256 * 1024;
constexpr size_t W_X4 = W_X1 + 4096;

constexpr unsigned SPIN_LDS = 1u << 22, SPIN_MEM = 1u << 17;

#define RLX_AGENT __ATOMIC_RELAXED, __HIP_MEMORY_SCOPE_AGENT

__device__ __forceinline__ float rbf(float x) { return (float)(__bf16)x; }
__device__ __forceinline__ float lo16(unsigned v) { return __uint_as_float(v << 16); }
__device__ __forceinline__ float hi16(unsigned v) { return __uint_as_float(v & 0xffff0000u); }
__device__ __forceinline__ unsigned pack2(float a, float b) { bf16x2 v = {(__bf16)a, (__bf16)b}; return __builtin_bit_cast(unsigned, v); }
__device__ __forceinline__ float dot2(unsigned a, unsigned b, float c) {
  return __builtin_amdgcn_fdot2_f32_bf16(__builtin_bit_cast(bf16x2, a), __builtin_bit_cast(bf16x2, b), c, false);
}
__device__ __forceinline__ unsigned pair_lo(unsigned r0, unsigned r1) { return __builtin_amdgcn_perm(r1, r0, 0x05040100u); }
__device__ __forceinline__ unsigned pair_hi(unsigned r0, unsigned r1) { return __builtin_amdgcn_perm(r1, r0, 0x07060302u); }
template <int CTRL> __device__ __forceinline__ float dpp(float x) {
  return __builtin_bit_cast(float, __builtin_amdgcn_mov_dpp(__builtin_bit_cast(int, x), CTRL, 0xf, 0xf, true));
}
constexpr int XOR1 = 0xB1, XOR2 = 0x4E, HMIR = 0x141, ROR4 = 0x124, ROR8 = 0x128, BCAST = 0x150;
__device__ __forceinline__ float x16_add(float x) { auto s = __builtin_amdgcn_permlane16_swap(__float_as_uint(x), __float_as_uint(x), false, false); return __uint_as_float(s[0]) + __uint_as_float(s[1]); }
__device__ __forceinline__ float x32_add(float x) { auto s = __builtin_amdgcn_permlane32_swap(__float_as_uint(x), __float_as_uint(x), false, false); return __uint_as_float(s[0]) + __uint_as_float(s[1]); }
__device__ __forceinline__ float s32_add(float a, float b) { auto s = __builtin_amdgcn_permlane32_swap(__float_as_uint(a), __float_as_uint(b), false, false); return __uint_as_float(s[0]) + __uint_as_float(s[1]); }
__device__ __forceinline__ float s16_add(float a, float b) { auto s = __builtin_amdgcn_permlane16_swap(__float_as_uint(a), __float_as_uint(b), false, false); return __uint_as_float(s[0]) + __uint_as_float(s[1]); }
__device__ __forceinline__ f32x2 rows_scatter(const float (&a)[8]) {
  const float b0 = s32_add(a[0], a[4]), b1 = s32_add(a[1], a[5]), b2 = s32_add(a[2], a[6]), b3 = s32_add(a[3], a[7]);
  return f32x2{s16_add(b0, b2), s16_add(b1, b3)};
}
__device__ __forceinline__ float oct_scatter(const float (&a)[8], int lane) {
  const f32x2 t = rows_scatter(a);
  return ((lane & 8) ? t.y : t.x) + dpp<ROR8>((lane & 8) ? t.x : t.y);
}
__device__ __forceinline__ float x16_max(float x) { auto s = __builtin_amdgcn_permlane16_swap(__float_as_uint(x), __float_as_uint(x), false, false); return fmaxf(__uint_as_float(s[0]), __uint_as_float(s[1])); }
__device__ __forceinline__ float x32_max(float x) { auto s = __builtin_amdgcn_permlane32_swap(__float_as_uint(x), __float_as_uint(x), false, false); return fmaxf(__uint_as_float(s[0]), __uint_as_float(s[1])); }
__device__ __forceinline__ float row_sum(float x) { x += dpp<XOR1>(x); x += dpp<XOR2>(x); x += dpp<HMIR>(x); x += dpp<ROR8>(x); return x; }
__device__ __forceinline__ float row_max(float x) { x = fmaxf(x, dpp<XOR1>(x)); x = fmaxf(x, dpp<XOR2>(x)); x = fmaxf(x, dpp<HMIR>(x)); x = fmaxf(x, dpp<ROR8>(x)); return x; }
__device__ __forceinline__ float wave_sum(float x) { return x32_add(x16_add(row_sum(x))); }
__device__ __forceinline__ float wave_max(float x) { return x32_max(x16_max(row_max(x))); }
__device__ __forceinline__ unsigned next_tag(unsigned e) { return __builtin_amdgcn_readfirstlane(e) * 1664525u + 1013904223u; }
__device__ __forceinline__ void put(gu64* g, unsigned tag, float v) { __hip_atomic_store(g, ((u64)tag << 32) | __float_as_uint(v), RLX_AGENT); }
template <int N, int SLEEP> __device__ __forceinline__ void gather(gu64* g, int S, unsigned tag, float (&v)[N], bool ok = false) {
#pragma unroll 1
  for (unsigned spins = 0; spins < SPIN_MEM; ++spins) {
    if (!ok) {
      ok = true;
#pragma unroll
      for (int k = 0; k < N; ++k) { const u64 x = __hip_atomic_load(g + k * S, RLX_AGENT); v[k] = __uint_as_float((unsigned)x); ok &= (unsigned)(x >> 32) == tag; }
    }
    if (__all(ok)) break;
    if (SLEEP) __builtin_amdgcn_s_sleep(SLEEP);
  }
}
__device__ __forceinline__ unsigned lds_peek(unsigned addr) {
  unsigned v; asm volatile("ds_read_b32 %0, %1\n\ts_waitcnt lgkmcnt(0)" : "=v"(v) : "v"(addr) : "memory");
  return __builtin_amdgcn_readfirstlane(v);
}
template <int SLEEP> __device__ __forceinline__ unsigned lds_wait_ge(unsigned addr, unsigned want) {
  unsigned v = lds_peek(addr);
#pragma unroll 1
  for (unsigned spins = 0; v < want && spins < SPIN_LDS; ++spins) { __builtin_amdgcn_s_sleep(SLEEP); v = lds_peek(addr); }
  return v < want ? want : v;
}
__device__ __forceinline__ void lds_post(unsigned addr, unsigned v) { asm volatile("ds_write_b32 %0, %1" :: "v"(addr), "v"(v) : "memory"); }
__device__ __forceinline__ void lds_arrive(unsigned addr, int lane) {
  if (lane == 0) asm volatile("ds_add_u32 %0, %1" :: "v"(addr), "v"(1u) : "memory");
}
__device__ __forceinline__ void wg_barrier() { asm volatile("s_waitcnt lgkmcnt(0)\n\ts_barrier" ::: "memory"); }
__device__ __forceinline__ __amdgpu_buffer_rsrc_t rsrc(const void* p, unsigned bytes) { return __builtin_amdgcn_make_buffer_rsrc((void*)p, 0, bytes, 0x00020000); }

__device__ __forceinline__ void ln_stats(const unsigned (&xr)[5], float& mb, float& rinv) {
  float s = 0.f;
#pragma unroll
  for (int i = 0; i < 5; ++i) s += lo16(xr[i]) + hi16(xr[i]);
  const float mean = wave_sum(s) * (1.f / D);
  float q = 0.f;
#pragma unroll
  for (int i = 0; i < 5; ++i) { const float a = lo16(xr[i]) - mean, b = hi16(xr[i]) - mean; q += a * a + b * b; }
  mb = rbf(mean);
  rinv = __builtin_amdgcn_rcpf(rbf(__builtin_amdgcn_sqrtf(rbf(rbf(wave_sum(q) * (1.f / D)) + 1e-5f))));
}
__device__ __forceinline__ float ln_apply(float v, float mb, float rinv) { return rbf(rbf(v - mb) * rinv); }

__global__ void __launch_bounds__(NTHR, 2)
fused_decode(const u16* __restrict__ x, const u16* __restrict__ kc, const u16* __restrict__ vc, const u16* __restrict__ wq,
             const u16* __restrict__ wo, const u16* __restrict__ w1, const u16* __restrict__ w3, const u16* __restrict__ w2,
             u16* __restrict__ out, unsigned char* ws_) {
  __shared__ __attribute__((aligned(16))) unsigned char smem[L_END];
  LAS unsigned char* lds = (LAS unsigned char*)smem;
  const unsigned lbase = (unsigned)(size_t)lds;
  GAS unsigned char* ws = (GAS unsigned char*)ws_;
  const int lane = threadIdx.x & 63, wave = __builtin_amdgcn_readfirstlane(threadIdx.x >> 6);
  const int b = blockIdx.x, h = (b & 7) * 4 + (b >> 6), s = (b >> 3) & 7, wg = h * NS + s;

  {
    unsigned t0, t1;
    asm volatile("s_getpc_b64 s[40:41]\nLcode_0:\n\ts_add_u32 m0, %3, %4\n\tv_lshlrev_b32 %0, 6, %2\n\tv_min_u32 %0, Lcode_e-Lcode_0, %0\n\t"
                 "global_load_lds_dword %0, s[40:41]\n\t"
                 "v_and_b32 %0, 7, %2\n\tv_lshl_add_u32 %0, %0, 2, %3\n\tv_mov_b32 %1, 0\n\tv_add_u32 %0, %5, %0\n\tds_write_b32 %0, %1"
                 : "=&v"(t0), "=&v"(t1) : "v"(threadIdx.x), "s"(lbase), "i"(L_FFN), "i"(L_DONE) : "s40", "s41", "scc", "memory");
  }
  asm volatile("" :: "s"(x), "s"(kc), "s"(vc), "s"(wq), "s"(wo), "s"(w1), "s"(w3), "s"(w2), "s"(out), "s"(ws));

  {
    asm volatile(
      "s_cmp_eq_u32 %[wave], 0\n\ts_cbranch_scc1 Lload%=\n\t"
      "s_waitcnt lgkmcnt(0)\n\ts_barrier\n\ts_branch Lskip%=\n"
      "Lload%=:\n\t"
      "s_lshl_b32 s54, %[u0], 5\n\ts_add_u32 s36, %[klo], s54\n\ts_addc_u32 s37, %[khi], 0\n\ts_and_b32 s37, s37, 0xffff\n\t"
      "s_mov_b32 s38, %[nrec]\n\ts_mov_b32 s39, 0x20000\n\ts_mov_b32 m0, %[ring]\n\ts_or_b32 s46, %[u0], %[sbase]\n\tv_lshlrev_b32 v10, 4, %[tid]\n\t"
      ".rept 4\n\t"
      "buffer_load_dwordx4 v10, s[36:39], s46 offen nt lds\n\t"
      "buffer_load_dwordx4 v10, s[36:39], s46 offen offset:1024 nt lds\n\t"
      "buffer_load_dwordx4 v10, s[36:39], s46 offen offset:2048 nt lds\n\t"
      "buffer_load_dwordx4 v10, s[36:39], s46 offen offset:3072 nt lds\n\t"
      "s_addk_i32 m0, 0x1000\n\ts_addk_i32 s46, 0x1000\n\t"
      ".endr\n\t"
      "s_add_u32 s47, %[u0], 0x20000\n\ts_and_b32 s46, s47, 0x7e0000\n\ts_or_b32 s46, s46, %[sbase]\n\t"
      ".rept 2\n\t"
      "buffer_load_dwordx4 v10, s[36:39], s46 offen nt lds\n\t"
      "buffer_load_dwordx4 v10, s[36:39], s46 offen offset:1024 nt lds\n\t"
      "buffer_load_dwordx4 v10, s[36:39], s46 offen offset:2048 nt lds\n\t"
      "buffer_load_dwordx4 v10, s[36:39], s46 offen offset:3072 nt lds\n\t"
      "s_addk_i32 m0, 0x1000\n\ts_addk_i32 s46, 0x1000\n\t"
      ".endr\n\t"
      "s_memrealtime s[44:45]\n\ts_setprio 3\n\t"
      "s_add_u32 s57, %[vlo], s54\n\ts_addc_u32 s58, %[vhi], 0\n\ts_and_b32 s58, s58, 0xffff\n\t"
      "s_add_u32 s53, %[ring], %[done]\n\tv_mov_b32 v11, s53\n\tv_add_u32 v12, 16, v11\n\t"
      "v_mov_b32 v4, 0\n\tv_mov_b32 v5, 0\n\tv_mov_b32 v6, 0\n\tv_mov_b32 v7, 0\n\tv_mov_b32 v9, 0\n\t"
      "s_getpc_b64 s[40:41]\n"
      "Lanchor%=:\n\t"
      "s_add_u32 s40, s40, Ltab%=-Lanchor%=\n\ts_addc_u32 s41, s41, 0\n\t"
      "s_mov_b32 s48, 2\n\ts_mov_b32 s49, 0x4000\n\ts_mov_b32 s50, 0\n\t"
      "s_waitcnt lgkmcnt(0)\n\ts_barrier\n\t"
      "s_mov_b32 s52, s44\n\ts_mov_b32 s56, %[center]\n\t"
      "Ltail%=:\n\t"
      "s_waitcnt lgkmcnt(0)\n\t"
      "ds_write_b32 v12, v9\n\t"
      "v_min_u32 v6, v6, v7\n\tv_min3_u32 v4, v4, v5, v6\n\t"
      "s_sub_u32 s53, s44, s52\n\ts_lshr_b32 s54, s50, 16\n\ts_sub_i32 s53, s53, s54\n\ts_sub_i32 s53, s53, %[toff]\n\t"
      "v_readfirstlane_b32 s55, v4\n\t"
      "s_and_b32 s54, s48, 7\n\ts_cbranch_scc1 Lnojump%=\n\ts_cmp_lt_u32 s48, %[m0p]\n\ts_cbranch_scc1 Lnojump%=\n\t"
      "s_sub_i32 s54, s53, s56\n\ts_cmp_gt_i32 s54, %[jump]\n\ts_cselect_b32 s54, s54, %[keep]\n\ts_sub_i32 s54, s54, %[keep]\n\t"
      "s_add_u32 s52, s52, s54\n\ts_sub_i32 s53, s53, s54\n\ts_mov_b32 s56, s53\n"
      "Lnojump%=:\n\t"
      "s_max_i32 s53, s53, 0\n\ts_min_i32 s53, s53, %[lagmax]\n\ts_mul_i32 s53, s53, %[lagk]\n\ts_lshr_b32 s53, s53, 16\n\t"
      "s_cmp_lt_u32 s48, %[m0p]\n\ts_cselect_b32 s51, %[idx0], s53\n\t"
      "s_lshl_b32 s50, s48, 8\n\ts_add_u32 s50, s50, 0x100\n\ts_mul_i32 s50, s50, %[pace]\n\t"
      "s_add_u32 s48, s48, 1\n\t"
      "s_cmp_eq_u32 s48, 0x80\n\ts_cselect_b32 s36, s57, s36\n\ts_cselect_b32 s37, s58, s37\n\t"
      "s_cmp_eq_u32 s48, 0x100\n\ts_cbranch_scc1 Lexit%=\n\t"
      "s_memrealtime s[44:45]\n\t"
      "ds_read_b128 v[4:7], v11\n\t"
      "s_add_u32 s55, s55, 7\n\ts_lshr_b32 s54, s48, 1\n\ts_cmp_ge_u32 s55, s54\n\ts_cbranch_scc0 Lspin%=\n"
      "Lroom%=:\n\t"
      "s_add_u32 s47, s47, 0x10000\n\ts_add_u32 s49, s49, 0x2000\n\ts_and_b32 s49, s49, 0x1ffff\n\t"
      "s_and_b32 s46, s47, 0x7e0000\n\ts_lshr_b32 s54, s47, 3\n\ts_and_b32 s54, s54, 0x2000\n\ts_or_b32 s46, s46, %[sbase]\n\ts_or_b32 s46, s46, s54\n\t"
      "s_add_u32 m0, s49, %[ring]\n\t"
      "s_lshl_b32 s54, s48, 3\n\ts_sub_i32 s54, s54, s51\n\ts_add_i32 s54, s54, -12\n\ts_ashr_i32 s54, s54, 2\n\t"
      "v_max_i32_e64 v8, s54, 0\n\ts_add_i32 s54, s54, 1\n\tv_max_i32_e64 v9, s54, 0\n\t"
      "s_lshl_b32 s54, s51, 7\n\ts_add_u32 s42, s40, s54\n\ts_addc_u32 s43, s41, 0\n\t"
      "s_setpc_b64 s[42:43]\n"
      "Lspin%=:\n\t"
      "s_mov_b32 s53, 0\n"
      "Lspin2%=:\n\t"
      "s_waitcnt lgkmcnt(0)\n\tv_min_u32 v6, v6, v7\n\tv_min3_u32 v4, v4, v5, v6\n\ts_add_u32 s53, s53, 1\n\tv_readfirstlane_b32 s55, v4\n\t"
      "ds_read_b128 v[4:7], v11\n\t"
      "s_cmp_gt_u32 s53, %[cap]\n\ts_cbranch_scc1 Lroom%=\n\t"
      "s_add_u32 s55, s55, 7\n\ts_cmp_ge_u32 s55, s54\n\ts_cbranch_scc0 Lspin2%=\n\ts_branch Lroom%=\n"
      ".p2align 7\n"
      "Ltab%=:\n\t"
      ".irp W,16,17,18,19,20,21,22,23,24,25\n\t"
      "buffer_load_dwordx4 v10, s[36:39], s46 offen nt lds\n\ts_waitcnt vmcnt(\\W)\n\t"
      "buffer_load_dwordx4 v10, s[36:39], s46 offen offset:1024 nt lds\n\ts_waitcnt vmcnt(\\W)\n\t"
      "buffer_load_dwordx4 v10, s[36:39], s46 offen offset:2048 nt lds\n\ts_waitcnt vmcnt(\\W)\n\t"
      "buffer_load_dwordx4 v10, s[36:39], s46 offen offset:3072 nt lds\n\t"
      "s_addk_i32 m0, 0x1000\n\ts_addk_i32 s46, 0x1000\n\ts_waitcnt vmcnt(\\W)\n\t"
      "ds_write_b32 v12, v8\n\t"
      "buffer_load_dwordx4 v10, s[36:39], s46 offen nt lds\n\ts_waitcnt vmcnt(\\W)\n\t"
      "buffer_load_dwordx4 v10, s[36:39], s46 offen offset:1024 nt lds\n\ts_waitcnt vmcnt(\\W)\n\t"
      "buffer_load_dwordx4 v10, s[36:39], s46 offen offset:2048 nt lds\n\ts_waitcnt vmcnt(\\W)\n\t"
      "buffer_load_dwordx4 v10, s[36:39], s46 offen offset:3072 nt lds\n\ts_waitcnt vmcnt(\\W)\n\t"
      "s_branch Ltail%=\n\t"
      ".p2align 7\n\t"
      ".endr\n"
      "Lexit%=:\n\t"
      ".irp N,6,5,4,3,2,1,0\n\t"
      "v_mov_b32 v8, 512-\\N\n\ts_waitcnt vmcnt(4*\\N)\n\tds_write_b32 v12, v8\n\t"
      ".endr\n\t"
      "s_endpgm\n"
      "Lskip%=:"
      :
      : [wave] "s"(wave), [tid] "v"(threadIdx.x), [klo] "s"((unsigned)(u64)kc), [khi] "s"((unsigned)((u64)kc >> 32)), [vlo] "s"((unsigned)(u64)vc), [vhi] "s"((unsigned)((u64)vc >> 32)),
        [nrec] "i"(HEADB), [u0] "s"(h << 18), [sbase] "s"(s << 14), [ring] "s"(lbase + L_RING), [done] "i"(L_DONE - L_RING),
        [idx0] "i"(WIN - W_MIN), [toff] "i"(PACE_O + LAG0), [lagmax] "i"((W_MAX - W_MIN) * LAG_SH), [lagk] "i"((65536 + LAG_SH - 1) / LAG_SH),
        [m0p] "i"(PACE_M0), [pace] "i"(PACE_T), [cap] "i"(SPIN_LDS), [jump] "i"(JUMP), [keep] "i"(KEEP), [center] "i"(CENTER)
      : "memory", "scc", "vcc", "s36", "s37", "s38", "s39", "s40", "s41", "s42", "s43", "s44", "s45", "s46", "s47", "s48", "s49", "s50", "s51", "s52", "s53", "s54", "s55", "s56", "s57", "s58",
        "v4", "v5", "v6", "v7", "v8", "v9", "v10", "v11", "v12");
  }

  unsigned xr[5];
#pragma unroll
  for (int i = 0; i < 5; ++i) xr[i] = ((const GAS unsigned*)x)[lane + 64 * i];
  unsigned ep = __hip_atomic_load((gu32*)(ws + W_EPOCH), RLX_AGENT);
  const int r = lane >> 4, c = lane & 15, p = lane >> 3, j = lane & 7;
#define PIN_X() asm volatile("" : "+v"(xr[0]), "+v"(xr[1]), "+v"(xr[2]), "+v"(xr[3]), "+v"(xr[4]), "+v"(ep))

  if (__builtin_expect(wave == 5, 0)) {
    PIN_X();
    const unsigned tag = next_tag(ep);
    gu64* x0 = (gu64*)(ws + W_REC) + h * (NS * 128) + 64;
    if (h < FG) {
      float mb, rinv, gs[8];
      ln_stats(xr, mb, rinv);
      LAS __bf16* xn = (LAS __bf16*)(lds + L_R2);
#pragma unroll
      for (int i = 0; i < 10; ++i) {
        const int e = 2 * (lane + 64 * (i >> 1)) + (i & 1);
        xn[(e & 7) * 80 + (e >> 4) * 2 + ((e >> 3) & 1)] = (__bf16)ln_apply((i & 1) ? hi16(xr[i >> 1]) : lo16(xr[i >> 1]), mb, rinv);
      }
      const __amdgpu_buffer_rsrc_t rw = rsrc(s < 4 ? w1 : w3, D * F * 2);
      const unsigned vo = (160 * (s & 3) + p) * (F * 2) + 128 * h + 16 * j;
#pragma unroll
      for (int k = 0; k < 8; ++k) gs[k] = 0.f;
      {
        lds_wait_ge<4>(lbase + L_FULL, 4 * GU_AT);
        u32x4 a[20];
#pragma unroll
        for (int i = 0; i < 20; ++i) a[i] = __builtin_amdgcn_raw_buffer_load_b128(rw, vo, i * (8 * F * 2), 2);
        __builtin_amdgcn_sched_barrier(0);
#pragma unroll
        for (int i = 0; i < 10; ++i) {
          const unsigned xq = *(LAS unsigned*)(xn + 80 * p + 20 * (s & 3) + 2 * i);
#pragma unroll
          for (int k = 0; k < 4; ++k) { gs[2 * k] = dot2(pair_lo(a[2 * i][k], a[2 * i + 1][k]), xq, gs[2 * k]); gs[2 * k + 1] = dot2(pair_hi(a[2 * i][k], a[2 * i + 1][k]), xq, gs[2 * k + 1]); }
        }
      }
      put(x0 + s * 128 + lane, tag, oct_scatter(gs, lane));
    }
    lds_wait_ge<32>(lbase + L_FULL + 8, 4);
    if (h < FG && s < CG) {
      float v[8];
      gather<8, 32>(x0 + lane, 128, tag, v);
      const float ga = rbf((v[0] + v[1]) + (v[2] + v[3])), ua = rbf((v[4] + v[5]) + (v[6] + v[7]));
      ((LAS __bf16*)(lds + L_HV))[8 * j + p] = (__bf16)(rbf(ga / (1.f + __expf(-ga))) * ua);
    }
    const f32x2 ml = *(LAS f32x2*)(lds + L_ML + 8 * (lane & 3));
    float mg = fmaxf(ml.x, dpp<XOR1>(ml.x)); mg = fmaxf(mg, dpp<XOR2>(mg));
    float lg = ml.y * __builtin_amdgcn_exp2f(ml.x - mg); lg += dpp<XOR1>(lg); lg += dpp<XOR2>(lg);
    gu64* x1 = (gu64*)(ws + W_X1) + h * 16;
    if (lane < 2) put(x1 + 2 * s + lane, tag, lane ? lg : mg);
    lds_wait_ge<32>(lbase + L_FULL, 4 * 84);
    float e[1] = {0.f};
    if (lane < 16) gather<1, 32>(x1 + lane, 1, tag, e);
    const float en = dpp<XOR1>(e[0]), em = (lane & 1) ? en : e[0], el = (lane & 1) ? e[0] : en;
    float M = fmaxf(em, dpp<XOR2>(em)); M = fmaxf(M, dpp<ROR8>(M)); M = fmaxf(M, dpp<ROR4>(M));
    float L = el * __builtin_amdgcn_exp2f(em - M); L += dpp<XOR2>(L); L += dpp<ROR8>(L); L += dpp<ROR4>(L);
    if (lane < 4) *(LAS float*)(lds + L_CW + 4 * lane) = __builtin_amdgcn_exp2f(ml.x - M) / L;
    {
      unsigned lo, hi, len;
      asm volatile("s_getpc_b64 s[40:41]\nLcode_p:\n\ts_add_u32 %0, s40, Lcode_a-Lcode_p\n\ts_addc_u32 %1, s41, (Lcode_a-Lcode_p)>>32\n\ts_mov_b32 %2, Lcode_b-Lcode_a" : "=s"(lo), "=s"(hi), "=s"(len) :: "s40", "s41", "scc");
      const GAS unsigned char* pc = (const GAS unsigned char*)(((u64)hi << 32) | lo);
      unsigned t = 0;
      if (lane * 64 < (int)len) t = *(const GAS unsigned*)(pc + lane * 64);
      asm volatile("" :: "v"(t));
    }
    if (s >= CG) return;
    const u32x4 h0 = *(LAS u32x4*)(lds + L_HV + 32 * r), h1 = *(LAS u32x4*)(lds + L_HV + 32 * r + 16);
    wg_barrier();
    float f[8];
#pragma unroll
    for (int k = 0; k < 8; ++k) f[k] = 0.f;
    if (h < FG) {
      const __amdgpu_buffer_rsrc_t r2 = rsrc(w2, F * D * 2);
      const unsigned vo2 = (64 * h + 16 * r) * (D * 2) + 256 * s + 16 * c;
      __builtin_amdgcn_s_sleep(3);
      u32x4 wd[16];
#pragma unroll
      for (int i = 0; i < 16; ++i) wd[i] = __builtin_amdgcn_raw_buffer_load_b128(r2, vo2, i * (D * 2), 2);
      __builtin_amdgcn_sched_barrier(0);
#pragma unroll
      for (int i = 0; i < 8; ++i) {
        const unsigned hp = i < 4 ? h0[i & 3] : h1[i & 3];
#pragma unroll
        for (int k = 0; k < 4; ++k) { f[2 * k] = dot2(pair_lo(wd[2 * i][k], wd[2 * i + 1][k]), hp, f[2 * k]); f[2 * k + 1] = dot2(pair_hi(wd[2 * i][k], wd[2 * i + 1][k]), hp, f[2 * k + 1]); }
      }
    }
    *(LAS f32x2*)(lds + L_FFN + 32 * c + 8 * r) = rows_scatter(f);
    wg_barrier();
    asm volatile("Lcode_e:");
    return;
  }

  const int cw = wave - 1;
  const unsigned a_full = lbase + L_FULL, a_done = lbase + L_DONE + 4 * cw;
  LAS unsigned char* ring = lds + L_RING + cw * 4096 + lane * 16;
  LAS float* sc = (LAS float*)(lds + L_SC) + cw * 1024 + lane;
  unsigned have = 0;
  u32x4 k0[NPRE][4];
  unsigned qp[4], tag;
  {
    const int row0 = 160 * (s >> 1) + 40 * cw + 5 * p;
    const __amdgpu_buffer_rsrc_t rq = rsrc(wq, D * H * DH * 2);
    const unsigned vo = row0 * (H * DH * 2) + h * (DH * 2) + (s & 1) * 128 + j * 16;
    unsigned xrow[5];
    u32x4 w[5];
#pragma unroll
    for (int i = 0; i < 5; ++i) xrow[i] = ((const GAS u16*)x)[row0 + i];
#pragma unroll
    for (int i = 0; i < 5; ++i) w[i] = __builtin_amdgcn_raw_buffer_load_b128(rq, vo, i * (H * DH * 2), 2);
    __builtin_amdgcn_sched_barrier(0);
    PIN_X();
    tag = next_tag(ep);
    float mb, rinv;
    ln_stats(xr, mb, rinv);
    float a[8];
#pragma unroll
    for (int k = 0; k < 8; ++k) a[k] = 0.f;
#pragma unroll
    for (int i = 0; i < 5; ++i) {
      const float xv = ln_apply(lo16(xrow[i]), mb, rinv);
#pragma unroll
      for (int k = 0; k < 4; ++k) { a[2 * k] = fmaf(xv, lo16(w[i][k]), a[2 * k]); a[2 * k + 1] = fmaf(xv, hi16(w[i][k]), a[2 * k + 1]); }
    }
    ((LAS float*)(lds + L_QP))[cw * 64 + 8 * j + p] = oct_scatter(a, lane);
    lds_arrive(lbase + L_FULL + 4, lane);
    gu64* xq = (gu64*)(ws + W_REC) + h * (NS * 128);
    if (cw == 0) {
      lds_wait_ge<0>(lbase + L_FULL + 4, 4);
      LAS float* qf = (LAS float*)(lds + L_QP) + lane;
      put(xq + s * 128 + lane, tag, (qf[0] + qf[64]) + (qf[128] + qf[192]));
    }
    gu64* gq = xq + (2 * r + (cw >> 1)) * 128 + 32 * (cw & 1) + 2 * c;
    u64 fq[2];
#pragma unroll
    for (int u = 0; u < NPRE; ++u) {
      if (u == XQ_AT) { fq[0] = __hip_atomic_load(gq, RLX_AGENT); fq[1] = __hip_atomic_load(gq + 1, RLX_AGENT); __builtin_amdgcn_sched_barrier(0); }
      if (have < (unsigned)(4 * u + cw + 1)) have = lds_wait_ge<1>(a_full, 4 * u + cw + 1);
#pragma unroll
      for (int i = 0; i < 4; ++i) k0[u][i] = *(LAS u32x4*)(ring + u * 16384 + i * 1024);
      lds_post(a_done, u + 1);
    }
    float g2[2] = {__uint_as_float((unsigned)fq[0]), __uint_as_float((unsigned)fq[1])};
    const bool okq = (unsigned)(fq[0] >> 32) == tag && (unsigned)(fq[1] >> 32) == tag;
    if (!__all(okq)) gather<2, 0>(gq, 1, tag, g2, okq);
    const float q0 = x32_add(x16_add(g2[0])), q1 = x32_add(x16_add(g2[1]));
    LAS unsigned* qb = (LAS unsigned*)(lds + L_QP + 1024);
    if (r == 0) qb[16 * cw + c] = pack2(q0, q1);
    lds_arrive(lbase + L_FULL + 12, lane);
    lds_wait_ge<0>(lbase + L_FULL + 12, 4);
    const u32x4 t = *(LAS u32x4*)(qb + 4 * c);
    qp[0] = t.x; qp[1] = t.y; qp[2] = t.z; qp[3] = t.w;
  }
  const int col = (s < CG ? 128 * s : 0) + 4 * h + 2 * (lane & 1);
  unsigned xv = ((const GAS unsigned*)x)[col >> 1];
  const bool b0 = c & 1, b1 = c & 2;
  auto score4 = [&](const u32x4 (&kk)[4]) -> float {
    float a[4];
#pragma unroll
    for (int i = 0; i < 4; ++i) a[i] = dot2(kk[i].w, qp[3], dot2(kk[i].z, qp[2], dot2(kk[i].y, qp[1], dot2(kk[i].x, qp[0], 0.f))));
    const float r01 = (b0 ? a[1] : a[0]) + dpp<XOR1>(b0 ? a[0] : a[1]);
    const float r23 = (b0 ? a[3] : a[2]) + dpp<XOR1>(b0 ? a[2] : a[3]);
    float t = (b1 ? r23 : r01) + dpp<XOR2>(b1 ? r01 : r23);
    t += dpp<ROR8>(t); t += dpp<ROR4>(t);
    return t;
  };
  auto scale = [](float v) -> float { return rbf(rbf(v) * 0.08838834764831845f) * 1.4426950408889634f; };
  float mx = -INFINITY;
#pragma unroll
  for (int G = 0; G < NPRE / 4; ++G) {
    const float t0 = score4(k0[4 * G]), t1 = score4(k0[4 * G + 1]), t2 = score4(k0[4 * G + 2]), t3 = score4(k0[4 * G + 3]);
    const float s2 = scale((c & 8) ? ((c & 4) ? t3 : t2) : ((c & 4) ? t1 : t0));
    mx = fmaxf(mx, s2);
    sc[G * 64] = s2;
  }
#pragma unroll 1
  for (int G = NPRE / 4; G < 16; ++G) {
    float sreg = 0.f;
#pragma unroll
    for (int u = 0; u < 4; ++u) {
      const int g = 4 * G + u;
      if (have < (unsigned)(4 * g + cw + 1)) have = lds_wait_ge<1>(a_full, 4 * g + cw + 1);
      u32x4 kk[4];
#pragma unroll
      for (int i = 0; i < 4; ++i) kk[i] = *(LAS u32x4*)(ring + (g & 7) * 16384 + i * 1024);
      lds_post(a_done, g + 1);
      const float t = score4(kk);
      sreg = (c >> 2) == u ? t : sreg;
    }
    const float s2 = scale(sreg);
    mx = fmaxf(mx, s2);
    sc[G * 64] = s2;
  }
  mx = wave_max(mx);
  {
    float l = 0.f;
#pragma unroll
    for (int G = 0; G < 16; ++G) l += __builtin_amdgcn_exp2f(sc[G * 64] - mx);
    l = wave_sum(l);
    if (lane == 0) *(LAS f32x2*)(lds + L_ML + 8 * cw) = f32x2{mx, l};
    lds_arrive(lbase + L_FULL + 8, lane);
  }
  asm volatile("" : "+v"(xv));
  float acc[8];
#pragma unroll
  for (int k = 0; k < 8; ++k) acc[k] = 0.f;
  unsigned vo_wo = (DH * h + 32 * cw + 8 * r) * (D * 2) + 256 * s + 16 * c;
  const int tt = (cw & 1) * 64 + lane;
  gu64* const x3 = (gu64*)(ws + W_REC);
  tag = next_tag(tag);
  gu64* const x4 = (gu64*)(ws + W_X4) + (s < CG ? s : 0) * (H * 128);
  gu64 *p3 = x3 + wg * 128 + tt, *g3 = x3 + (h * NS + (c >> 1)) * 128 + 32 * cw + 2 * r + (c & 1);
  gu64 *p4 = x4 + h * 128 + tt, *g4 = x4 + (lane >> 1) * 128 + 4 * h + 2 * (lane & 1);
  gu32* po = (gu32*)out + (col >> 1);
  unsigned a_part = lbase + L_QP + 4 * ((tt & ~31) + 8 * ((tt >> 1) & 3) + 4 * (tt & 1) + ((tt >> 3) & 3)), a_red = lbase + cw * 512 + 32 * c + 8 * r;
  asm volatile("" : "+v"(vo_wo), "+v"(p3), "+v"(g3), "+v"(p4), "+v"(g4), "+v"(po), "+v"(a_part), "+v"(a_red));
#pragma unroll 1
  for (int G = 0; G < 16; ++G) {
    const float preg = __builtin_amdgcn_exp2f(sc[G * 64] - mx);
#define PV_SLOT(u) { \
      const int g = 64 + 4 * G + (u); \
      if (have < (unsigned)(4 * g + cw + 1)) have = lds_wait_ge<1>(a_full, 4 * g + cw + 1); \
      u32x4 vv[4]; \
      _Pragma("unroll") for (int i = 0; i < 4; ++i) vv[i] = *(LAS u32x4*)(ring + (g & 7) * 16384 + i * 1024); \
      lds_post(a_done, g + 1); \
      const float pb[4] = {dpp<BCAST + 4 * (u)>(preg), dpp<BCAST + 4 * (u) + 1>(preg), dpp<BCAST + 4 * (u) + 2>(preg), dpp<BCAST + 4 * (u) + 3>(preg)}; \
      _Pragma("unroll") for (int i = 0; i < 4; ++i) \
        _Pragma("unroll") for (int k = 0; k < 4; ++k) { acc[2 * k] = fmaf(pb[i], lo16(vv[i][k]), acc[2 * k]); acc[2 * k + 1] = fmaf(pb[i], hi16(vv[i][k]), acc[2 * k + 1]); } \
      asm volatile("" : "+v"(acc[0]), "+v"(acc[1]), "+v"(acc[2]), "+v"(acc[3]), "+v"(acc[4]), "+v"(acc[5]), "+v"(acc[6]), "+v"(acc[7])); }
    PV_SLOT(0) PV_SLOT(1) PV_SLOT(2) PV_SLOT(3)
  }
#undef PV_SLOT
  asm volatile("Lcode_a:");
  *(LAS f32x2*)(size_t)(a_red + L_QP) = rows_scatter(acc);
  wg_barrier();
  if (cw < 2) {
    const f32x4 cs = *(LAS f32x4*)(lds + L_CW);
    LAS float* part = (LAS float*)(size_t)a_part;
    put(p3, tag, fmaf(cs.w, part[384], fmaf(cs.z, part[256], fmaf(cs.y, part[128], cs.x * part[0]))));
  }
  if (s < CG) {
    if (cw < 2) __builtin_amdgcn_s_sleep(6); else __builtin_amdgcn_s_sleep(10);
    const __amdgpu_buffer_rsrc_t ro = rsrc(wo, H * DH * D * 2);
    u32x4 wr[8];
#pragma unroll
    for (int i = 0; i < 8; ++i) wr[i] = __builtin_amdgcn_raw_buffer_load_b128(ro, vo_wo, i * (D * 2), 2);
    __builtin_amdgcn_sched_barrier(0);
    float o[4];
    __builtin_amdgcn_s_sleep(4);
    u64 f3[4];
#pragma unroll
    for (int k = 0; k < 4; ++k) f3[k] = __hip_atomic_load(g3 + 8 * k, RLX_AGENT);
    __builtin_amdgcn_sched_barrier(0);
    unsigned wp[4][8];
#pragma unroll
    for (int i = 0; i < 4; ++i)
#pragma unroll
      for (int k = 0; k < 4; ++k) {
        wp[i][2 * k] = pair_lo(wr[2 * i][k], wr[2 * i + 1][k]); wp[i][2 * k + 1] = pair_hi(wr[2 * i][k], wr[2 * i + 1][k]);
        asm volatile("" : "+v"(wp[i][2 * k]), "+v"(wp[i][2 * k + 1]));
      }
    __builtin_amdgcn_sched_barrier(0);
    bool ok3 = true;
#pragma unroll
    for (int k = 0; k < 4; ++k) { o[k] = __uint_as_float((unsigned)f3[k]); ok3 &= (unsigned)(f3[k] >> 32) == tag; }
    if (!__all(ok3)) gather<4, 0>(g3, 8, tag, o, ok3);
#pragma unroll
    for (int k = 0; k < 4; ++k) { o[k] += dpp<XOR2>(o[k]); o[k] += dpp<ROR8>(o[k]); o[k] += dpp<ROR4>(o[k]); }
    const unsigned p0 = pack2(o[0], o[1]), p1 = pack2(o[2], o[3]);
    const unsigned n0 = __builtin_amdgcn_mov_dpp(p0, XOR1, 0xf, 0xf, true), n1 = __builtin_amdgcn_mov_dpp(p1, XOR1, 0xf, 0xf, true);
    const bool odd = c & 1;
    const unsigned op[4] = {odd ? n0 : p0, odd ? n1 : p1, odd ? p0 : n0, odd ? p1 : n1};
    float y[8];
#pragma unroll
    for (int k = 0; k < 8; ++k) y[k] = dot2(wp[3][k], op[3], dot2(wp[2][k], op[2], dot2(wp[1][k], op[1], dot2(wp[0][k], op[0], 0.f))));
    *(LAS f32x2*)(size_t)(a_red + L_R2) = rows_scatter(y);
    wg_barrier();
    if (cw < 2) {
      LAS float* r2 = (LAS float*)(lds + L_R2) + tt;
      put(p4, tag, ((r2[0] + r2[128]) + (r2[256] + r2[384])) + ((LAS float*)(lds + L_FFN))[tt]);
    }
    if (cw == 2) {
      float z[2];
      gather<2, 0>(g4, 1, tag, z);
      if (wg == 0 && lane == 0) __hip_atomic_store((gu32*)(ws + W_EPOCH), tag, RLX_AGENT);
#pragma unroll
      for (int k = 0; k < 2; ++k) { z[k] += dpp<XOR2>(z[k]); z[k] += dpp<ROR8>(z[k]); z[k] += dpp<ROR4>(z[k]); z[k] = x32_add(x16_add(z[k])); }
      if (lane < 2) *po = pack2(lo16(xv) + z[0], hi16(xv) + z[1]);
    }
  }
  asm volatile("Lcode_b:");
  asm volatile("s_endpgm");
}

extern "C" void kernel_launch(void* const* d_in, const int* in_sizes, int n_in, void* d_out, int out_size, void* d_ws, size_t ws_size,
                              hipStream_t stream) {
  fused_decode<<<H * NS, NTHR, 0, stream>>>((const u16*)d_in[0], (const u16*)d_in[1], (const u16*)d_in[2], (const u16*)d_in[3], (const u16*)d_in[4],
                                            (const u16*)d_in[6], (const u16*)d_in[7], (const u16*)d_in[8], (u16*)d_out, (unsigned char*)d_ws);
}
```
